```python
import torch

def _softmax(x, axis=-1):
    return torch.softmax(x, dim=axis)
def _gelu(x):
    return torch.nn.functional.gelu(x, approximate="tanh")
def _silu(x):
    return torch.nn.functional.silu(x)
def _sigmoid(x):
    return torch.sigmoid(x)
def _layernorm(x, axis=-1, eps=1e-5):
    m = x.mean(dim=axis, keepdim=True)
    v = x.var(dim=axis, keepdim=True, unbiased=False)
    return (x - m) / torch.sqrt(v + eps)
def _scatter_add(tgt, idx, src, axis=0):
    return tgt.clone().index_add_(axis, idx, src)
def _segment_sum(src, seg, n):
    out = torch.zeros((n,) + src.shape[1:], dtype=src.dtype)
    return out.index_add_(0, seg, src)
def _pad(x, pw):
    flat = tuple(v for pair in reversed(pw) for v in pair)
    return torch.nn.functional.pad(x, flat)

kernel_name = 'standalone__deep_cross_wide_deep__recsys'

def setup_inputs():

    _w = (lambda a: (lambda t: t.to(torch.bfloat16) if t.is_floating_point() else t)(torch.as_tensor(a)))
    B, F, D = 4096, 100, 64
    V, Nd = min(100000, 100000), 13
    Din = F * D
    H = 4 * D
    return {
        "idx": _w((lambda lo, hi, *s: torch.randint(lo, hi, s))(0, V, B, F).to(torch.int64)),
        "table": _w((lambda *s: torch.randn(*s, dtype=torch.bfloat16) * 1.6)(V, D) * 0.05),
        "x_wide": _w((lambda *s: torch.randn(*s, dtype=torch.bfloat16) * 1.6)(B, Nd)),
        "w_wide": _w((lambda *s: torch.randn(*s, dtype=torch.bfloat16) * 1.6)(Nd) * 0.05),
        "b_wide": _w((lambda *s: torch.randn(*s, dtype=torch.bfloat16) * 1.6)(1) * 0.01),
        "W1": _w((lambda *s: torch.randn(*s, dtype=torch.bfloat16) * 1.6)(Din, H) * (2.0 / Din) ** 0.5),
        "b1": _w((lambda *s: torch.randn(*s, dtype=torch.bfloat16) * 1.6)(H) * 0.01),
        "W2": _w((lambda *s: torch.randn(*s, dtype=torch.bfloat16) * 1.6)(H) * (1.0 / H) ** 0.5),
        "b2": _w((lambda *s: torch.randn(*s, dtype=torch.bfloat16) * 1.6)(1) * 0.01),
    }

def reference(idx, table, x_wide, w_wide, b_wide, W1, b1, W2, b2):
    emb = table[idx]
    deep_in = emb.reshape(emb.shape[0], -1)
    h = deep_in @ W1 + b1
    h = h * (lambda x, dt: x.to(dt))(h > 0, h.dtype)
    deep_logit = h @ W2 + b2
    wide_logit = x_wide @ w_wide + b_wide
    return _sigmoid(wide_logit + deep_logit)
```

```cpp
#include <hip/hip_runtime.h>
#include <hip/hip_bf16.h>
#include <stdint.h>

typedef unsigned long long u64;
using s16x4 = __attribute__((ext_vector_type(4))) short;
using s16x8 = __attribute__((ext_vector_type(8))) short;
using f32x4 = __attribute__((ext_vector_type(4))) float;
using f32x2 = __attribute__((ext_vector_type(2))) float;
using b16x2 = __attribute__((ext_vector_type(2))) __bf16;
using u32x4 = __attribute__((ext_vector_type(4))) unsigned;
typedef __attribute__((address_space(3))) s16x4* lds_s16x4_p;
typedef __attribute__((address_space(1))) u64 gu64;

namespace {

constexpr int kB = 4096, kF = 100, kH = 256, kNd = 13;
constexpr int kNA = 3;
constexpr int kSlot = 13 * 2048;
constexpr int kWD = 2;
constexpr int kLdsA = 0;
constexpr int kLdsW = kNA * kSlot;
constexpr int kLdsIdx = kLdsW + 8 * (kWD + 1) * 2048;
constexpr int kLdsB1 = kLdsIdx + 8192;
constexpr int kLdsW2 = kLdsB1 + 1024;
constexpr int kLdsWide = kLdsW2 + 1024;
constexpr int kLdsRed = kLdsWide + 1024;
constexpr int kLdsTag = kLdsRed + 512;
constexpr int kLdsJunk = kLdsTag + 64;
constexpr int kLdsBytes = kLdsJunk + 256;

constexpr size_t kWsPart = 0;
constexpr size_t kWsPartBytes = (size_t)32 * 8 * 8 * 8192;
constexpr size_t kWsFlag = kWsPartBytes;
constexpr size_t kWsEpoch = kWsFlag + 32 * 8 * 128;
constexpr size_t kWsBytes = kWsEpoch + 32 * 128;
constexpr u64 kTagStep = 0x9E3779B97F4A7C15ull;

__device__ __forceinline__ void dma16(const void* src, char* lds, unsigned off) {
  __builtin_amdgcn_global_load_lds((const unsigned*)src, (unsigned*)(lds + off), 16, 0, 0);
}
__device__ __forceinline__ void dma4(const void* src, char* lds, unsigned off) {
  __builtin_amdgcn_global_load_lds((const unsigned*)src, (unsigned*)(lds + off), 4, 0, 0);
}
template <int N> __device__ __forceinline__ void waitvm() { asm volatile("s_waitcnt vmcnt(%0)" ::"i"(N) : "memory"); }
template <int N> __device__ __forceinline__ void waitlgkm() { asm volatile("s_waitcnt lgkmcnt(%0)" ::"i"(N) : "memory"); }
__device__ __forceinline__ void bar() { asm volatile("" ::: "memory"); __builtin_amdgcn_s_barrier(); asm volatile("" ::: "memory"); }
__device__ __forceinline__ float bf2f(unsigned short v) { return __uint_as_float((unsigned)v << 16); }
__device__ __forceinline__ unsigned pack2(float a, float b) {
  f32x2 v = {a, b};
  b16x2 r = __builtin_convertvector(v, b16x2);
  return __builtin_bit_cast(unsigned, r);
}

__global__ __launch_bounds__(512, 2) void wide_deep_kernel(
    const int* __restrict__ idx, const char* __restrict__ table, const unsigned short* __restrict__ x_wide,
    const unsigned short* __restrict__ w_wide, const unsigned short* __restrict__ b_wide,
    const char* __restrict__ W1, const unsigned short* __restrict__ b1, const unsigned short* __restrict__ W2,
    const unsigned short* __restrict__ b2, unsigned short* __restrict__ out, char* ws, unsigned vmax, unsigned pad_) {
  extern __shared__ __attribute__((aligned(16))) char lds[];
  const int tid = threadIdx.x, lane = tid & 63;
  const int w = __builtin_amdgcn_readfirstlane(tid >> 6);
  const int s = blockIdx.x & 7, mt = blockIdx.x >> 3;
  const int odd = s & 1;
  const int FB = (25 * s) >> 1;
  const int F0 = FB + odd;
  const int FH = odd ? FB : FB + 12;
  const int rot = mt % 12;

  const int t2 = tid - 256;
  const unsigned short* p0 = tid < 256 ? b1 + tid : (t2 < 208 ? x_wide + (mt * 128 + 16 * s) * kNd + t2 : (t2 == 208 ? b2 : b_wide));
  const unsigned short* p1 = tid < 256 ? W2 + tid : w_wide + (t2 % kNd);
  unsigned sv0, sv1;
  u64 ep;
  asm volatile("global_load_ushort %0, %1, off" : "=v"(sv0) : "v"(p0) : "memory");
  asm volatile("global_load_ushort %0, %1, off" : "=v"(sv1) : "v"(p1) : "memory");
  asm volatile("global_load_dwordx2 %0, %1, off sc1" : "=v"(ep) : "v"(ws + kWsEpoch + mt * 128 + (lane & 7) * 8) : "memory");

  {
    const int cc = lane & 15, q = cc >> 2, j = cc & 3;
    int p = 3 * q + j + rot; p = p >= 12 ? p - 12 : p;
    const int col = j < 3 ? F0 + p : FH;
#pragma unroll
    for (int i = 0; i < 4; ++i) {
      const int g = w * 4 + i;
      dma4(idx + (mt * 128 + 4 * g + (lane >> 4)) * kF + col, lds, kLdsIdx + g * 256);
    }
  }
  const unsigned wring = kLdsW + w * ((kWD + 1) * 2048);
  const unsigned wsrc = (lane >> 2) * 512 + w * 64 + (((lane & 3) ^ ((lane >> 4) & 2)) * 16);
  auto issueW = [&](int t) {
    int krow;
    if (t < 24) { int p = (t >> 1) + rot; p = p >= 12 ? p - 12 : p; krow = (F0 + p) * 64 + (t & 1) * 32; }
    else krow = FH * 64 + odd * 32;
    const char* base = W1 + (size_t)krow * 512;
    dma16(base + wsrc, lds, wring + (t % (kWD + 1)) * 2048);
    dma16(base + 8192 + wsrc, lds, wring + (t % (kWD + 1)) * 2048 + 1024);
  };
  dma4(W1 + (size_t)(FH * 64 + odd * 32 + mt) * 512 + (lane & 3) * 128, lds, kLdsJunk);
#pragma unroll
  for (int t = 0; t < kWD; ++t) issueW(t);

  const unsigned asrc = (((lane & 7) ^ ((4 * (w & 1) + (lane >> 4)) & 7)) * 16);
  const unsigned aidx = kLdsIdx + (8 * (w & 1) + (lane >> 3)) * 64 + (w >> 1) * 16;
  const unsigned adst = (3 * (w >> 1)) * 2048 + (w & 1) * 1024;
  auto loadIdx = [&](int i) {
    const int o = (s + 1 + i) & 7;
    u32x4 ix = *(const u32x4*)(lds + aidx + o * 1024);
#pragma unroll
    for (int j = 0; j < 4; ++j) ix[j] = min(ix[j], vmax) * 128u + asrc;
    return ix;
  };
  auto issueA = [&](const u32x4& ix, int j, unsigned slot) {
    if (j < 3) dma16(table + ix[j], lds, slot + adst + j * 2048);
    else if (w < 2) dma16(table + ix[3], lds, slot + 12 * 2048 + (w & 1) * 1024);
  };

  s16x8 Wf[25][2];
  {
    const int g = lane >> 4, q = (lane & 15) >> 2, p = lane & 3;
    const unsigned lbase = (unsigned)(size_t)(__attribute__((address_space(3))) char*)lds;
    const unsigned r0 = lbase + wring + (8 * g + q) * 64 + (((p >> 1) ^ (2 * (g & 1))) * 16) + 8 * (p & 1);
    const unsigned r1 = r0 ^ 32;
#pragma unroll
    for (int t = 0; t < 25; ++t) {
      if (t + kWD <= 25) waitvm<2 * (kWD - 1)>();
      else waitvm<3>();
      const int so = (t % (kWD + 1)) * 2048;
      s16x4 a0, a1, c0, c1;
      asm volatile("ds_read_b64_tr_b16 %0, %4 offset:%6\n\tds_read_b64_tr_b16 %1, %4 offset:%7\n\t"
                   "ds_read_b64_tr_b16 %2, %5 offset:%6\n\tds_read_b64_tr_b16 %3, %5 offset:%7"
                   : "=&v"(a0), "=&v"(a1), "=&v"(c0), "=&v"(c1)
                   : "v"(r0), "v"(r1), "i"(so), "i"(so + 256) : "memory");
      Wf[t][0] = __builtin_shufflevector(a0, a1, 0, 1, 2, 3, 4, 5, 6, 7);
      Wf[t][1] = __builtin_shufflevector(c0, c1, 0, 1, 2, 3, 4, 5, 6, 7);
      waitlgkm<4>();
      if (t + kWD < 25) issueW(t + kWD);
      if (t == 0) {
        asm volatile("" : "+v"(sv0), "+v"(sv1), "+v"(ep));
        const float f0 = __uint_as_float(sv0 << 16), f1 = __uint_as_float(sv1 << 16);
        if (tid < 256) { *(float*)(lds + kLdsB1 + tid * 4) = f0; *(float*)(lds + kLdsW2 + tid * 4) = f1; }
        else *(float*)(lds + kLdsWide + t2 * 4) = t2 < 208 ? f0 * f1 : f0;
        if (tid < 8) *(u64*)(lds + kLdsTag + tid * 8) = ep + kTagStep;
      }
      if (t == 22) bar();
      if (t >= 23) {
        const u32x4 ix = loadIdx(t - 23);
#pragma unroll
        for (int j = 0; j < 4; ++j) issueA(ix, j, kLdsA + (t - 23) * kSlot);
      }
    }
  }

  waitlgkm<0>();
  __builtin_amdgcn_sched_barrier(0);

  const auto prs = __builtin_amdgcn_make_buffer_rsrc(ws + kWsPart, 0, (int)kWsPartBytes, 0x00020000);
  const unsigned ard = (lane & 15) * 128 + (((lane >> 4) ^ ((lane >> 1) & 7)) * 16);
  f32x4 acc0, acc1;
  auto compute = [&](unsigned slot, bool pf, int inext, unsigned nslot) {
    const unsigned e0 = slot + ard, e1 = slot + (ard ^ 64), eh = odd ? e1 : e0;
    u32x4 ix;
    if (pf) ix = loadIdx(inext);
    acc0 = f32x4{0.f, 0.f, 0.f, 0.f}; acc1 = acc0;
#pragma unroll
    for (int t = 0; t < 25; ++t) {
      const unsigned ad = (t == 24 ? eh : ((t & 1) ? e1 : e0)) + (t >> 1) * 2048;
      const s16x8 a = *(const s16x8*)(lds + ad);
      acc0 = __builtin_amdgcn_mfma_f32_16x16x32_bf16(Wf[t][0], a, acc0, 0, 0, 0);
      acc1 = __builtin_amdgcn_mfma_f32_16x16x32_bf16(Wf[t][1], a, acc1, 0, 0, 0);
      if (pf && (t == 1 || t == 7 || t == 13 || t == 19)) issueA(ix, (t - 1) / 6, nslot);
    }
  };
  auto storeP = [&](int i) {
    const int o = (s + 1 + i) & 7;
    u32x4 v = {pack2(acc0[0], acc0[1]), pack2(acc0[2], acc0[3]), pack2(acc1[0], acc1[1]), pack2(acc1[2], acc1[3])};
    __builtin_amdgcn_raw_buffer_store_b128(v, prs, (((mt * 8 + o) * 8 + s) * 8 + w) * 1024 + lane * 16, 0, 16);
  };
  auto publish = [&](int i) {
    if (tid == 448) {
      const int o = (s + 1 + i) & 7;
      const u64 tag = *(const u64*)(lds + kLdsTag + o * 8);
      __hip_atomic_store((gu64*)(ws + kWsFlag + (mt * 8 + o) * 128) + s, tag, __ATOMIC_RELAXED, __HIP_MEMORY_SCOPE_AGENT);
    }
  };

  unsigned slot = kLdsA, nslot = kLdsA + 2 * kSlot;
#pragma unroll 1
  for (int i = 0; i < 6; ++i) {
    if (i == 0) { if (w < 2) waitvm<4>(); else waitvm<3>(); }
    else { if (w < 2) waitvm<5>(); else waitvm<4>(); }
    bar();
    if (i >= 2) publish(i - 2);
    compute(slot, true, i + 2, nslot);
    storeP(i);
    nslot = slot;
    slot = slot + kSlot >= kLdsA + kNA * kSlot ? kLdsA : slot + kSlot;
  }
  if (w < 2) waitvm<5>(); else waitvm<4>();
  bar();
  publish(4);
  compute(slot, false, 0, 0);
  storeP(6);
  slot = slot + kSlot >= kLdsA + kNA * kSlot ? kLdsA : slot + kSlot;
  waitvm<1>();
  bar();
  publish(5);
  compute(slot, false, 0, 0);
  waitvm<0>();
  bar();
  publish(6);

  {
    const u64 tag = *(const u64*)(lds + kLdsTag + s * 8);
    gu64* fl = (gu64*)(ws + kWsFlag + (mt * 8 + s) * 128) + (lane & 7);
    for (int spin = 0; spin < (1 << 22); ++spin) {
      const u64 v = __hip_atomic_load(fl, __ATOMIC_RELAXED, __HIP_MEMORY_SCOPE_AGENT);
      if (__all(v == tag || (lane & 7) == s)) break;
      __builtin_amdgcn_s_sleep(2);
    }
    asm volatile("" ::: "memory");
    u32x4 pv[7];
#pragma unroll
    for (int j = 0; j < 7; ++j) {
      const int src = (s + 1 + j) & 7;
      pv[j] = __builtin_amdgcn_raw_buffer_load_b128(prs, (((mt * 8 + s) * 8 + src) * 8 + w) * 1024 + lane * 16, 0, 16);
    }
    float h[8] = {acc0[0], acc0[1], acc0[2], acc0[3], acc1[0], acc1[1], acc1[2], acc1[3]};
#pragma unroll
    for (int j = 0; j < 7; ++j) {
#pragma unroll
      for (int e = 0; e < 4; ++e) {
        h[2 * e] += __uint_as_float(pv[j][e] << 16);
        h[2 * e + 1] += __uint_as_float(pv[j][e] & 0xffff0000u);
      }
    }
    const int n0 = w * 32 + (lane >> 4) * 4;
    const f32x4 bA = *(const f32x4*)(lds + kLdsB1 + n0 * 4), bB = *(const f32x4*)(lds + kLdsB1 + n0 * 4 + 64);
    const f32x4 uA = *(const f32x4*)(lds + kLdsW2 + n0 * 4), uB = *(const f32x4*)(lds + kLdsW2 + n0 * 4 + 64);
    float d = 0.f;
#pragma unroll
    for (int e = 0; e < 4; ++e) {
      d += fmaxf(h[e] + bA[e], 0.f) * uA[e];
      d += fmaxf(h[4 + e] + bB[e], 0.f) * uB[e];
    }
    d += __shfl_xor(d, 16);
    d += __shfl_xor(d, 32);
    if (lane < 16) *(float*)(lds + kLdsRed + (w * 16 + lane) * 4) = d;
    __syncthreads();
    if (tid < 16) {
      float z = *(const float*)(lds + kLdsWide + 208 * 4) + *(const float*)(lds + kLdsWide + 209 * 4);
#pragma unroll
      for (int k = 0; k < 8; ++k) z += *(const float*)(lds + kLdsRed + (k * 16 + tid) * 4);
#pragma unroll
      for (int k = 0; k < kNd; ++k) z += *(const float*)(lds + kLdsWide + (tid * kNd + k) * 4);
      const float r = 1.f / (1.f + __expf(-z));
      out[mt * 128 + 16 * s + tid] = (unsigned short)(pack2(r, r) & 0xffffu);
    }
    if (tid == 64) __hip_atomic_store((gu64*)(ws + kWsEpoch + mt * 128) + s, tag, __ATOMIC_RELAXED, __HIP_MEMORY_SCOPE_AGENT);
  }
}

}

extern "C" void kernel_launch(void* const* d_in, const int* in_sizes, int n_in, void* d_out, int out_size,
                              void* d_ws, size_t ws_size, hipStream_t stream) {
  if (ws_size < kWsBytes) return;
  const unsigned vmax = (unsigned)(in_sizes[1] / 64 - 1);
  (void)hipFuncSetAttribute((const void*)wide_deep_kernel, hipFuncAttributeMaxDynamicSharedMemorySize, kLdsBytes);
  wide_deep_kernel<<<256, 512, kLdsBytes, stream>>>(
      (const int*)d_in[0], (const char*)d_in[1], (const unsigned short*)d_in[2], (const unsigned short*)d_in[3],
      (const unsigned short*)d_in[4], (const char*)d_in[5], (const unsigned short*)d_in[6], (const unsigned short*)d_in[7],
      (const unsigned short*)d_in[8], (unsigned short*)d_out, (char*)d_ws, vmax, 0u);
}
```

```cpp
#include <hip/hip_runtime.h>
#include <stdint.h>

typedef unsigned long long u64;
using s16x4 = __attribute__((ext_vector_type(4))) short;
using s16x8 = __attribute__((ext_vector_type(8))) short;
using f32x4 = __attribute__((ext_vector_type(4))) float;
using f32x2 = __attribute__((ext_vector_type(2))) float;
using b16x2 = __attribute__((ext_vector_type(2))) __bf16;
using u32x4 = __attribute__((ext_vector_type(4))) unsigned;
using u16x2 = __attribute__((ext_vector_type(2))) unsigned short;
using u32x2 = __attribute__((ext_vector_type(2))) unsigned;

#define GB -1
#define GS 6
#define IPS 3
#define TD3 23
#define TDY 22
#define XSEL (mt & 1)
#define NTM 0
#define PS0 2
#define PSD 6
#define TS 5
#define TSO 1
#define TX0 4
#define TXD 3
#define TPL 17
#define J0 3
#define TQ0 2
#define TQ1 20
#define TCP 11
#define TCN 6
#define TCI 4
#define DOT 0
#define WF 1
#define ORD 0x04627531u
#define PR1 8
#define PR2 14
#define PR3 18
#define TP5 6
#define TP6A 14
#define TP6C 24
#define TP6B 20

namespace {

constexpr int kF = 100, kNd = 13;
constexpr int kSlot = 13 * 2048;
constexpr int kLdsW = 3 * kSlot;
constexpr int kLdsP = 4 * kSlot;
constexpr int kLdsIdx = kLdsW + 8 * 8192;
constexpr int kLdsBW = kLdsIdx + 8192;
constexpr int kLdsX = kLdsBW + 1024;
constexpr int kLdsWw = kLdsX + 1024;
constexpr int kLdsWide = kLdsWw + 256;
constexpr int kLdsRed = kLdsWide + 64;
constexpr int kLdsPf = kLdsRed + 512;
constexpr int kLdsBytes = kLdsPf + 1024;
static_assert(kLdsBytes <= 163840 && kLdsP + 4 * 8192 <= kLdsIdx, "lds");

#define CPM 1
#define BLK 65536
constexpr unsigned kBlk = BLK;
constexpr unsigned kWsPartBytes = 256 * kBlk;
constexpr unsigned kWsEpoch = kWsPartBytes;
constexpr unsigned kWsBytes = kWsEpoch + 32 * 128;
constexpr unsigned kTagBits = 0x00010001u;

__device__ __forceinline__ u32x4 mkrs(const void* p, unsigned bytes) {
  const u64 a = (u64)p;
  return u32x4{(unsigned)a, (unsigned)(a >> 32) & 0xffffu, bytes, 0x00020000u};
}
__device__ __forceinline__ void dmaB16(const u32x4& rs, unsigned voff, unsigned soff, unsigned dst) {
  asm volatile("s_mov_b32 m0, %3\n\ts_nop 0\n\tbuffer_load_dwordx4 %0, %1, %2 offen lds"
               :: "v"(voff), "s"(rs), "s"(soff), "s"(dst) : "memory");
}
#define GSFX "nt"
#define WSFX ""
__device__ __forceinline__ void dmaBW(const u32x4& rs, unsigned voff, unsigned soff, unsigned dst) {
  asm volatile("s_mov_b32 m0, %3\n\ts_nop 0\n\tbuffer_load_dwordx4 %0, %1, %2 offen " WSFX " lds"
               :: "v"(voff), "s"(rs), "s"(soff), "s"(dst) : "memory");
}
template <int NT> __device__ __forceinline__ void dmaBG(const u32x4& rs, unsigned voff, unsigned soff, unsigned dst) {
  if constexpr (NT) asm volatile("s_mov_b32 m0, %3\n\ts_nop 0\n\tbuffer_load_dwordx4 %0, %1, %2 offen " GSFX " lds"
               :: "v"(voff), "s"(rs), "s"(soff), "s"(dst) : "memory");
  else dmaB16(rs, voff, soff, dst);
}
template <int NT> __device__ __forceinline__ void dmaB16m(const u32x4& rs, unsigned voff, unsigned soff, unsigned dst, u64 mask) {
  if constexpr (NT) asm volatile("s_mov_b32 m0, %3\n\ts_mov_b64 exec, %4\n\tbuffer_load_dwordx4 %0, %1, %2 offen " GSFX " lds\n\ts_mov_b64 exec, -1"
               :: "v"(voff), "s"(rs), "s"(soff), "s"(dst), "s"(mask) : "memory");
  else asm volatile("s_mov_b32 m0, %3\n\ts_mov_b64 exec, %4\n\tbuffer_load_dwordx4 %0, %1, %2 offen lds\n\ts_mov_b64 exec, -1"
               :: "v"(voff), "s"(rs), "s"(soff), "s"(dst), "s"(mask) : "memory");
}
__device__ __forceinline__ void dmaB4(const u32x4& rs, unsigned voff, unsigned soff, unsigned dst) {
  asm volatile("s_mov_b32 m0, %3\n\ts_nop 0\n\tbuffer_load_dword %0, %1, %2 offen lds"
               :: "v"(voff), "s"(rs), "s"(soff), "s"(dst) : "memory");
}
__device__ __forceinline__ void dmaG16(const void* p, unsigned dst) {
  asm volatile("s_mov_b32 m0, %1\n\ts_nop 0\n\tglobal_load_lds_dwordx4 %0, off" :: "v"(p), "s"(dst) : "memory");
}
__device__ __forceinline__ void dmaG2(const void* p, unsigned dst) {
  asm volatile("s_mov_b32 m0, %1\n\ts_nop 0\n\tglobal_load_lds_ushort %0, off" :: "v"(p), "s"(dst) : "memory");
}
#define PSFX "sc1"
#define LSFX "sc1"
__device__ __forceinline__ void st16(const u32x4& v, const u32x4& rs, unsigned voff, unsigned soff) {
  asm volatile("buffer_store_dwordx4 %0, %1, %2, %3 offen " PSFX "\n\ts_nop 1"
               :: "v"(v), "v"(voff), "s"(rs), "s"(soff) : "memory");
}
__device__ __forceinline__ void stFlag(unsigned v, const u32x4& rs, unsigned soff) {
  asm volatile("s_mov_b64 exec, 1\n\tbuffer_store_dword %0, off, %1, %2 sc1\n\ts_mov_b64 exec, -1"
               :: "v"(v), "s"(rs), "s"(soff) : "memory");
}
__device__ __forceinline__ void ldPr(const u32x4& a, const u32x4& rs, unsigned voff, unsigned soff) {
  asm volatile("buffer_load_dwordx4 %0, %1, %2, %3 offen " LSFX :: "v"(a), "v"(voff), "s"(rs), "s"(soff) : "memory");
}
__device__ __forceinline__ void ldP(u32x4& a, const u32x4& rs, unsigned voff, unsigned soff) {
  asm volatile("buffer_load_dwordx4 %0, %1, %2, %3 offen " LSFX : "=v"(a) : "v"(voff), "s"(rs), "s"(soff) : "memory");
}
__device__ __forceinline__ void dmaP(const u32x4& rs, unsigned voff, unsigned soff, unsigned dst) {
  asm volatile("s_mov_b32 m0, %3\n\ts_nop 0\n\tbuffer_load_dwordx4 %0, %1, %2 offen " LSFX " lds"
               :: "v"(voff), "s"(rs), "s"(soff), "s"(dst) : "memory");
}
__device__ __forceinline__ void dmaPm(const u32x4& rs, unsigned voff, unsigned soff, unsigned dst, u64 mask) {
  asm volatile("s_mov_b32 m0, %3\n\ts_mov_b64 exec, %4\n\tbuffer_load_dwordx4 %0, %1, %2 offen " LSFX " lds\n\ts_mov_b64 exec, -1"
               :: "v"(voff), "s"(rs), "s"(soff), "s"(dst), "s"(mask) : "memory");
}
__device__ __forceinline__ void touch(const u32x4& rs, unsigned voff, unsigned soff, unsigned dst) {
  asm volatile("s_mov_b32 m0, %3\n\ts_nop 0\n\tbuffer_load_dword %0, %1, %2 offen sc1 lds"
               :: "v"(voff), "s"(rs), "s"(soff), "s"(dst) : "memory");
}
__device__ __forceinline__ void ld4(unsigned& v, const u32x4& rs, unsigned voff, unsigned soff) {
  asm volatile("buffer_load_dword %0, %1, %2, %3 offen sc1" : "=v"(v) : "v"(voff), "s"(rs), "s"(soff) : "memory");
}
template <int OFF, typename T> __device__ __forceinline__ void rd128(T& v, unsigned addr) {
  asm volatile("ds_read_b128 %0, %1 offset:%2" : "=v"(v) : "v"(addr), "i"(OFF) : "memory");
}
template <int OFF> __device__ __forceinline__ void rd64x2(u32x4& v, unsigned addr) {
  asm volatile("ds_read2_b64 %0, %1 offset0:%2 offset1:%3" : "=v"(v) : "v"(addr), "i"(OFF / 8), "i"(OFF / 8 + 4) : "memory");
}
template <int N> __device__ __forceinline__ void waitvm() { asm volatile("s_waitcnt vmcnt(%0)" ::"i"(N) : "memory"); }
template <int N> __device__ __forceinline__ void waitvm(u32x4& a, u32x4& b) { asm volatile("s_waitcnt vmcnt(%2)" : "+v"(a), "+v"(b) : "i"(N) : "memory"); }
template <int N, int M> __device__ __forceinline__ void waitvm2(int h) {
  if constexpr (N == M) waitvm<N>();
  else asm volatile("s_cmp_lg_u32 %0, 0\n\ts_cbranch_scc1 .Lwv%=\n\ts_waitcnt vmcnt(%1)\n.Lwv%=:\n\ts_waitcnt vmcnt(%2)"
                    :: "s"(h), "i"(N), "i"(M) : "scc", "memory");
}
template <int N> __device__ __forceinline__ void waitlgkm() { asm volatile("s_waitcnt lgkmcnt(%0)" ::"i"(N) : "memory"); }
template <int N, typename T> __device__ __forceinline__ void waitlgkm(T& x) { asm volatile("s_waitcnt lgkmcnt(%1)" : "+v"(x) : "i"(N) : "memory"); }
template <typename T> __device__ __forceinline__ void tie(T& x) { asm volatile("" : "+v"(x)); }
__device__ __forceinline__ void bar() { asm volatile("s_barrier" ::: "memory"); }
__device__ __forceinline__ void fence() { __builtin_amdgcn_sched_barrier(0); }
__device__ __forceinline__ unsigned pack2(float a, float b) {
  f32x2 v = {a, b};
  b16x2 r = __builtin_convertvector(v, b16x2);
  return __builtin_bit_cast(unsigned, r);
}
__device__ __forceinline__ float lo16(unsigned v) { return __uint_as_float(v << 16); }
__device__ __forceinline__ float hi16(unsigned v) { return __uint_as_float(v & 0xffff0000u); }
__device__ __forceinline__ unsigned tag0(unsigned n) { return (n & 1u) | ((n & 2u) << 15); }
__device__ __forceinline__ unsigned tag1(unsigned n) { return (~n & 1u) | ((n & 4u) << 14); }
__device__ __forceinline__ unsigned packT(float a, float b, unsigned t, unsigned keep) {
  u16x2 r = __builtin_bit_cast(u16x2, __builtin_amdgcn_perm(__float_as_uint(b), __float_as_uint(a), 0x07060302u));
  r += u16x2{1, 1};
  return (__builtin_bit_cast(unsigned, r) & keep) | t;
}
template <int N> __device__ __forceinline__ bool untag(u32x4& o, const u32x4& a, unsigned t0, unsigned t1) {
  asm volatile("s_waitcnt vmcnt(%2)" : "+s"(t0), "+s"(t1) : "i"(N) : "memory");
  o = u32x4{a[0] ^ t0, a[1] ^ t1, a[2] ^ t0, a[3] ^ t1};
  return __builtin_amdgcn_ballot_w64(((o[0] | o[1] | o[2] | o[3]) & kTagBits) != 0u) == 0ull;
}

constexpr int gstep(int g) { return GB + g / GS; }
constexpr int kT0 = gstep(3) < 0 ? 2 : gstep(3) + 4;
constexpr int kTag = kT0;
constexpr int gcount(int x, int t) {
  const int h = x >> 1;
  if (!(x & 1)) return (t == kTag) + (t == TDY ? 3 + h : 0);
  int n = 0;
  for (int g = 0; g < 6; ++g) if (gstep(g) == t) n += (g & 1) ? 1 + h : 2;
  return n + (t == kTag) + (t + 1 == TD3 ? 3 + h : 0);
}
constexpr int wcount(int x, int t) {
  if (t == 0) return 2 + gcount(x, -1);
  if (t == 1) return gcount(x, -1) + 4 + gcount(x, 0);
  if (t == 2) return 4 + gcount(x, 0) + gcount(x, 1);
  return gcount(x, t - 3) + (t + 1 < 25 ? 2 : 0) + gcount(x, t - 2) + (t + 2 < 25 ? 2 : 0) + gcount(x, t - 1);
}
constexpr int item0(int t) {
  if (t <= kT0) return 0;
  if (t > 25) return 50;
  const int a = IPS * (t - kT0), b = 2 * (t - 1);
  return a < b ? a : b;
}
constexpr int kIxA = TD3 - 2, kIxB = 24;
struct Sched {
  int item[50], ring[26];
  constexpr Sched() : item(), ring() {
    int n = 3, ar[53] = {0, 1, 2}, wr[26] = {};
    for (int t = 0; t < kT0; ++t) wr[t] = -1;
    for (int t = kT0; t <= 25; ++t) {
      ring[t] = n - 1 - wr[t - 1];
      if (t < 25) { n += 4; wr[t] = n - 1; }
      if (t == kIxA || t == kIxB) n += 1;
      for (int k = item0(t); k < item0(t + 1); ++k) {
        if (k + 3 < 50) ar[k + 3] = n++;
        const int p = ar[k] > wr[k >> 1] ? ar[k] : wr[k >> 1];
        item[k] = n - 1 - p > 15 ? 15 : n - 1 - p;
      }
    }
  }
};
constexpr Sched kS;
static_assert(gstep(5) + 4 <= TD3 && kT0 < kIxA && TD3 >= 23 && TD3 <= 24, "schedule");

constexpr int xread(int t) {
  return t >= TX0 && (t - TX0) % TXD == 0 && t < TX0 + 5 * TXD ? (t - TX0) / TXD : -1;
}
constexpr int qstep(int j) { return J0 == 4 ? TQ1 : TQ0 + (j - J0) * (TQ1 - TQ0) / (4 - J0); }
constexpr int xcount(int t) { return t < 0 ? 0 : (xread(t) >= 0 ? 1 : 0) + (t == 1 ? 2 : 0); }
constexpr int pcount(int b, int t) {
  return (CPM == 1 ? 1 : 3) * (TSO >= b && TSO < t) + (TP5 >= b && TP5 < t) + (TP6A >= b && TP6A < t) + (TP6B >= b && TP6B < t) + (TP6C >= b && TP6C < t);
}
static_assert(TX0 + 4 * TXD + 3 < 25 && TX0 > 1 && TSO < TP5 && TP5 < TP6A && TP6A < TP6B && TP6B < TP6C && TP6C < 25 && PS0 + 3 * PSD < 21 && TQ1 < 22 && TPL < 22, "steps");

template <int T> struct IC { static constexpr int v = T; };
template <int B, int E, typename Fn> __device__ __forceinline__ void sfor(Fn&& f) {
  if constexpr (B < E) { f(IC<B>{}); sfor<B + 1, E>(f); }
}

__global__ __launch_bounds__(512, 2) void wide_deep_kernel(
    const int* __restrict__ idx, const char* __restrict__ table, const char* __restrict__ x_wide,
    const char* __restrict__ w_wide, const char* __restrict__ b_wide, const char* __restrict__ W1,
    const char* __restrict__ b1, const char* __restrict__ W2, const char* __restrict__ b2,
    unsigned short* __restrict__ out, char* ws, unsigned tbytes, unsigned pad_) {
  extern __shared__ __attribute__((aligned(16))) char lds[];
  const int tid = threadIdx.x, lane = tid & 63;
  const int w = __builtin_amdgcn_readfirstlane(tid >> 6);
  const int s = blockIdx.x & 7, mt = blockIdx.x >> 3;
  const int odd = s & 1;
  const int FB = (25 * s) >> 1;
  const int F0 = FB + odd;
  const int FH = odd ? FB : FB + 12;
  const int rot = mt % 12;
  const int q4 = w >> 1, hw = w & 1;
  const u32x4 rsW = mkrs(W1, 6400u * 512u), rsT = mkrs(table, tbytes), rsI = mkrs(idx, 4096u * kF * 4u), rsP = mkrs(ws, kWsBytes);

  asm volatile(".Lwd_beg:\n\ts_nop 4");
  dmaBG<(NTM >> 5) & 1>(rsI, (((s + (ORD >> (lane >> 3) * 4)) & 7) * 16 + (lane & 7)) * (kF * 4), ((mt * 128 + hw * 8) * kF + FB + 3 * q4) * 4, kLdsIdx + w * 1024);
  if (w == 5) dmaG16((lane < 32 ? b1 : W2) + (lane & 31) * 16, kLdsBW);
  else if (w == 6) dmaG16(x_wide + (mt * 128 + 16 * s) * (kNd * 2) + min(lane, 25) * 16, kLdsX);
  else if (w == 3 || w == 4) {
    u64 pc; unsigned len, back;
    asm volatile("s_getpc_b64 %0\n.Lwd_pc:\n\ts_mov_b32 %1, .Lwd_end-.Lwd_beg+240\n\ts_mov_b32 %2, .Lwd_pc-.Lwd_beg" : "=s"(pc), "=s"(len), "=s"(back));
    dmaG16((const char*)((pc - back) & ~15ull) + min((unsigned)(((mt * 2 + w - 3) & 15) * 1024 + lane * 16), len & ~15u), kLdsPf);
  }
  else if (w == 7) dmaG2(lane < kNd ? w_wide + lane * 2 : (lane == kNd ? b2 : b_wide), kLdsWw);

  const unsigned wring = kLdsW + w * 2048;
  const unsigned wsrc = (lane >> 2) * 512 + w * 64 + (((lane & 3) ^ ((lane >> 4) & 2)) * 16);
  auto issueW = [&](int t) {
    int krow;
    if (t < 24) { int p = (t >> 1) + rot; p = p >= 12 ? p - 12 : p; krow = (F0 + p) * 64 + (t & 1) * 32; }
    else krow = FH * 64 + odd * 32;
    dmaBW(rsW, wsrc, krow * 512, wring + ((t + 3) & 3) * 16384);
    dmaBW(rsW, wsrc, krow * 512 + 8192, wring + ((t + 3) & 3) * 16384 + 1024);
  };
  issueW(0); issueW(1);

  const unsigned asrc = (((lane & 7) ^ ((4 * hw + (lane >> 4)) & 7)) * 16);
  const u64 hmask = (q4 == (odd ? 0 : 3)) ? __ballot((asrc & 64) == 0) : 0ull;
  const int h4 = ((8 - 7 * odd) >> q4) & 1;
  const u64 pm0 = odd ? hmask : ~0ull, pm3 = odd ? ~0ull : hmask;
  unsigned pd[4];
#pragma unroll
  for (int j = 0; j < 4; ++j) {
    int p = 3 * q4 + j - odd - rot; p = p < 0 ? p + 12 : p;
    if (j == (odd ? 0 : 3)) p = 12;
    pd[j] = p * 2048 + hw * 1024;
  }
  unsigned aidx = kLdsIdx + w * 1024 + (lane >> 3) * 16;
  auto issueA = [&](auto nc, const u32x4& ix, int j, unsigned slot) {
    constexpr int NT = (NTM >> decltype(nc)::v) & 1;
    if (j == 0) { if (pm0) dmaB16m<NT>(rsT, ix[0] * 128u + asrc, odd * 64, slot + pd[0], pm0); }
    else if (j == 3) { if (pm3) dmaB16m<NT>(rsT, ix[3] * 128u + asrc, 0, slot + pd[3], pm3); }
    else dmaBG<NT>(rsT, ix[j] * 128u + asrc, 0, slot + pd[j]);
  };

  s16x8 Wf[25][2];
  u32x4 ix;
  const unsigned l16 = lane * 16;
  unsigned e0 = (lane & 15) * 128 + (((lane >> 4) ^ ((lane >> 1) & 7)) * 16);
  unsigned e1 = e0 ^ 64;
  s16x8 a[5];
  f32x4 acc0, acc1, pac0, pac1;
  unsigned tagv;
  unsigned keep = ~kTagBits;
  unsigned sbase = mt * 8 * kBlk + (s * 8 + w) * 1024;
  asm volatile("" : "+s"(sbase));
  auto storeP = [&](int i, const f32x4& pac0, const f32x4& pac1, bool last = false) {
    const int o = (s + (ORD >> (i & 7) * 4)) & 7;
    const unsigned soff = sbase + o * kBlk;
    const unsigned tg = __builtin_amdgcn_readlane(tagv, o);
    const unsigned t0 = tag0(tg), t1 = tag1(tg);
    const u32x4 v = {packT(pac0[0], pac0[1], t0, keep), packT(pac0[2], pac0[3], t1, keep), packT(pac1[0], pac1[1], t0, keep), packT(pac1[2], pac1[3], t1, keep)};
    st16(v, rsP, l16, soff);
    const unsigned cb = CPM == 2 ? 256 * kBlk + (mt * 8 + o) * 16384 + w * 1024 : soff + (8 - s) * 8192;
    if (last && CPM != 1) { st16(v, rsP, l16, cb); st16(v, rsP, l16, cb + 8192); }
  };
  auto wide = [&]() {
    const int r = lane & 15, part = lane >> 4;
    unsigned x0, x1, x2, x3; u32x4 wv;
    const unsigned xa = kLdsX + r * (kNd * 2) + part * 8;
    asm volatile("ds_read_u16 %0, %5\n\tds_read_u16 %1, %5 offset:2\n\tds_read_u16 %2, %5 offset:4\n\tds_read_u16 %3, %5 offset:6\n\t"
                 "ds_read_b128 %4, %6\n\ts_waitcnt lgkmcnt(0)"
                 : "=&v"(x0), "=&v"(x1), "=&v"(x2), "=&v"(x3), "=&v"(wv) : "v"(xa), "v"(kLdsWw + part * 16) : "memory");
    if (part == 3) { x1 = 0x3f80u; x2 = 0x3f80u; x3 = 0u; }
    float z = lo16(x0) * lo16(wv[0]) + lo16(x1) * lo16(wv[1]) + lo16(x2) * lo16(wv[2]) + lo16(x3) * lo16(wv[3]);
    z += __shfl_xor(z, 16);
    z += __shfl_xor(z, 32);
    if (lane < 16) *(float*)(lds + kLdsWide + lane * 4) = z;
    waitlgkm<0>();
  };
  auto startup = [&](auto xc) {
    constexpr int X = decltype(xc)::v;
    u32x4 ixs[3];
    auto groups = [&](auto tc) {
      constexpr int t = decltype(tc)::v;
      sfor<0, 6>([&](auto gc) {
        constexpr int g = decltype(gc)::v;
        if constexpr (gstep(g) == t) {
          issueA(IC<0>{}, ixs[g >> 1], (g & 1) ? 0 : 1, (g >> 1) * kSlot);
          issueA(IC<0>{}, ixs[g >> 1], (g & 1) ? 3 : 2, (g >> 1) * kSlot);
        }
      });
    };
    auto rdI = [&](auto kc) {
      constexpr int k = decltype(kc)::v, u = k >> 1;
      if constexpr (k < 50) rd128<(k & 1) * kSlot + (u >> 1) * 2048>(a[k & 3], (u & 1) ? e1 : e0);
    };
    if constexpr (X) {
      waitvm<4>();
      rd128<0>(ixs[0], aidx); rd128<128>(ixs[1], aidx); rd128<256>(ixs[2], aidx);
      waitlgkm<0>();
      tie(ixs[0]); tie(ixs[1]); tie(ixs[2]);
      groups(IC<-1>{});
    }
    const int g = lane >> 4, q = (lane & 15) >> 2, p = lane & 3;
    const unsigned r0 = wring + (8 * g + q) * 64 + (((p >> 1) ^ (2 * (g & 1))) * 16) + 8 * (p & 1);
    const unsigned r1 = r0 ^ 32;
    sfor<0, 25 + X>([&](auto tc) {
      constexpr int t = decltype(tc)::v;
      waitlgkm<(!X || t <= kT0 ? 0 : (kS.ring[t] > 15 ? 15 : kS.ring[t]))>();
      if (X && t == TD3) {
        bar();
        tie(ix);
#pragma unroll
        for (int j = 0; j < 4; ++j) issueA(IC<1>{}, ix, j, 3 * kSlot);
      }
      if constexpr (t < 25) waitvm2<wcount(X, t), wcount(X + 2, t)>(h4);
      if (t == 0) issueW(2);
      if (t + 3 < 25) issueW(t + 3);
      if (X && t == kT0) {
        bar();
        if (w == 0) wide();
        fence();
        sfor<0, 3>(rdI);
      }
      if constexpr (t < 25) {
        constexpr int so = ((t + 3) & 3) * 16384;
        s16x4 a0, a1, c0, c1;
        asm volatile("ds_read_b64_tr_b16 %0, %4 offset:%6\n\tds_read_b64_tr_b16 %1, %4 offset:%7\n\t"
                     "ds_read_b64_tr_b16 %2, %5 offset:%6\n\tds_read_b64_tr_b16 %3, %5 offset:%7"
                     : "=&v"(a0), "=&v"(a1), "=&v"(c0), "=&v"(c1)
                     : "v"(r0), "v"(r1), "i"(so), "i"(so + 256) : "memory");
        Wf[t][0] = __builtin_shufflevector(a0, a1, 0, 1, 2, 3, 4, 5, 6, 7);
        Wf[t][1] = __builtin_shufflevector(c0, c1, 0, 1, 2, 3, 4, 5, 6, 7);
      }
      if (t == kTag) ld4(tagv, rsP, (lane & 7) * 16, kWsEpoch + mt * 128);
      if constexpr (X) {
        groups(tc);
        if (t == kIxA) rd128<384>(ix, aidx);
        if (t == kIxB) rd128<512>(ix, aidx);
        sfor<item0(t), item0(t + 1)>([&](auto kc) {
          constexpr int k = decltype(kc)::v, u = k >> 1;
          const f32x4 z = {0.f, 0.f, 0.f, 0.f};
          rdI(IC<k + 3>{});
          waitlgkm<kS.item[k]>(a[k & 3]);
          if (k & 1) {
            pac0 = __builtin_amdgcn_mfma_f32_16x16x32_bf16(Wf[u][0], a[k & 3], u == 0 ? z : pac0, 0, 0, 0);
            pac1 = __builtin_amdgcn_mfma_f32_16x16x32_bf16(Wf[u][1], a[k & 3], u == 0 ? z : pac1, 0, 0, 0);
          } else {
            acc0 = __builtin_amdgcn_mfma_f32_16x16x32_bf16(Wf[u][0], a[k & 3], u == 0 ? z : acc0, 0, 0, 0);
            acc1 = __builtin_amdgcn_mfma_f32_16x16x32_bf16(Wf[u][1], a[k & 3], u == 0 ? z : acc1, 0, 0, 0);
          }
          fence();
        });
      } else {
        if (t == TDY) {
          bar();
          tie(ix);
#pragma unroll
          for (int j = 0; j < 4; ++j) issueA(IC<2>{}, ix, j, 0);
        }
        if (t + 1 == TDY) rd128<0>(ix, aidx);
        if (t == 24) rd128<128>(ix, aidx);
      }
    });
    tie(tagv);
    tagv += 1;
    tie(keep);
    if constexpr (X) {
      aidx += 640;
      e0 += 2 * kSlot; e1 += 2 * kSlot;
      storeP(0, acc0, acc1);
    } else {
      waitlgkm<0>(ix);
#pragma unroll
      for (int j = 0; j < 4; ++j) issueA(IC<3>{}, ix, j, kSlot);
      rd128<256>(ix, aidx);
      aidx += 384;
      if (w == 0) wide();
      asm volatile("" : "=v"(pac0), "=v"(pac1));
      waitvm2<3, 4>(h4);
    }
    waitlgkm<0>(ix);
    fence();
  };
  const int xt = XSEL;
  if (xt) startup(IC<1>{}); else startup(IC<0>{});

  auto rdA = [&](auto tc) {
    constexpr int t = decltype(tc)::v;
    rd128<(t >> 1) * 2048>(a[t == 24 ? 4 : t & 3], (t & 1) ? e1 : e0);
  };
  auto mma = [&](auto tc) {
    constexpr int t = decltype(tc)::v;
    const f32x4 z = {0.f, 0.f, 0.f, 0.f};
    acc0 = __builtin_amdgcn_mfma_f32_16x16x32_bf16(Wf[t][0], a[t == 24 ? 4 : t & 3], t == 0 ? z : acc0, 0, 0, 0);
    acc1 = __builtin_amdgcn_mfma_f32_16x16x32_bf16(Wf[t][1], a[t == 24 ? 4 : t & 3], t == 0 ? z : acc1, 0, 0, 0);
  };
  const unsigned pbase = (mt * 8 + s) * kBlk + w * 1024;
  auto psrc = [&](int j) { return pbase + ((s - (ORD >> (j & 7) * 4)) & 7) * 8192; };
  unsigned redo = 0;
  const unsigned cp1 = CPM == 1 ? psrc(6) : CPM == 2 ? 256 * kBlk + (mt * 8 + s) * 16384 + w * 1024 : pbase + 8 * 8192, cp2 = CPM == 1 ? cp1 : cp1 + 8192;
  const u64 spare = __ballot(lane == (hw ? 0 : 4));
  const u64 lm0 = pm0 ? pm0 : spare, lm3 = pm3 ? pm3 : spare;
  u64 pc2; unsigned len2;
  asm volatile("s_getpc_b64 %0\n.Lwd_pc2:\n\ts_mov_b32 %1, .Lwd_end-.Lwd_pc2+240" : "=s"(pc2), "=s"(len2));
  const u32x4 rsC = mkrs((const char*)(pc2 & ~127ull), len2 & ~15u);
  bar();
  fence();
  sfor<0, 3>(rdA);
  __builtin_amdgcn_s_setprio(3);
  auto iter = [&](auto lc, int i) {
    constexpr int L = decltype(lc)::v;
    sfor<0, 22>([&](auto tc) {
      constexpr int t = decltype(tc)::v;
      if (t == PR1) __builtin_amdgcn_s_setprio(2);
      if (t == PR2) __builtin_amdgcn_s_setprio(1);
      if (t == PR3) __builtin_amdgcn_s_setprio(0);
      if (WF) waitlgkm<2>(a[t & 3]);
      rdA(IC<t + 3>{});
      if (!WF) waitlgkm<3>(a[t & 3]);
      if constexpr (L) {
        sfor<J0, 5>([&](auto jc) {
          constexpr int j = decltype(jc)::v;
          if (t == qstep(j)) dmaP(rsP, l16, psrc(j), (j == 4 ? 0 : kLdsP + j * 8192) + w * 1024);
        });
      } else {
        if constexpr (t >= PS0 && (t - PS0) % PSD == 0 && (t - PS0) / PSD < 4) {
          constexpr int j = (t - PS0) / PSD, NT = (NTM >> 4) & 1;
          const unsigned dslot = ((i + 2) & 3) * kSlot;
          if (j == 0) tie(ix);
          if (j == 0) dmaB16m<NT>(rsT, ix[0] * 128u + asrc, odd * 64, dslot + pd[0], lm0);
          else if (j == 3) dmaB16m<NT>(rsT, ix[3] * 128u + asrc, 0, dslot + pd[3], lm3);
          else dmaBG<NT>(rsT, ix[j] * 128u + asrc, 0, dslot + pd[j]);
        }
        if (t == TCP) dmaB16m<0>(rsC, l16 * 8 + w * (TCN * 128), 0, kLdsPf, i >= TCI ? (1ull << TCN) - 1 : 1ull);
        if (J0 && t == TPL) dmaPm(rsP, l16, psrc(i - 6 + J0), kLdsP + ((i + 2 + J0) & 3) * 8192 + w * 1024, i < 6 - J0 ? 1ull : ~0ull);
      }
      mma(tc);
      if (t == TS) storeP(i - 1, pac0, pac1);
      if (t == PS0 + 3 * PSD + 1 && !L) { rd128<0>(ix, aidx); aidx += 128; }
      fence();
    });
    waitvm<(L ? 6 - J0 : (J0 ? 6 : 5) + (TCP > 0))>();
    bar();
    __builtin_amdgcn_s_setprio(3);
    {
      const int step = i == 3 ? -3 * kSlot : kSlot;
      e0 += step; e1 += step;
    }
    fence();
    sfor<22, 25>([&](auto tc) {
      constexpr int t = decltype(tc)::v;
      if (WF) waitlgkm<2>(a[t == 24 ? 4 : t & 3]);
      rdA(IC<t - 22>{});
      if (!WF) waitlgkm<3>(a[t == 24 ? 4 : t & 3]);
      mma(tc);
      fence();
    });
    pac0 = acc0; pac1 = acc1;
  };
#pragma unroll 1
  for (int i = 2 * xt; i < 6; ++i) iter(IC<0>{}, i);
  iter(IC<1>{}, 6);

  const unsigned tag = __builtin_amdgcn_readlane(tagv, s);
  const unsigned t0 = tag0(tag), t1 = tag1(tag);
  u32x4 p5, p6a, p6b, p6c, xs[5], uv, bv, pv;
  f32x4 sum0, sum1;
  unsigned bad = 0;
  s16x8 sel0, sel1;
  float u[8], bb[8], d;
  auto addP = [&](const u32x4& v) {
    acc0 = __builtin_amdgcn_mfma_f32_16x16x32_bf16(sel0, __builtin_bit_cast(s16x8, v), acc0, 0, 0, 0);
    acc1 = __builtin_amdgcn_mfma_f32_16x16x32_bf16(sel1, __builtin_bit_cast(s16x8, v), acc1, 0, 0, 0);
  };
  auto addV = [&](f32x4& r, unsigned x, unsigned y) {
    asm volatile("v_dot2_f32_bf16 %0, %4, %6, %0\n\tv_dot2_f32_bf16 %1, %4, %7, %1\n\tv_dot2_f32_bf16 %2, %5, %6, %2\n\tv_dot2_f32_bf16 %3, %5, %7, %3"
                 : "+v"(r[0]), "+v"(r[1]), "+v"(r[2]), "+v"(r[3]) : "v"(x), "v"(y), "s"(0x3f80u), "s"(0x3f800000u));
  };
  const unsigned ba = kLdsBW + w * 64 + (lane >> 4) * 8;
  const unsigned sa = l16 + w * 1024, sb = sa + kLdsP;
  sfor<0, 25>([&](auto tc) {
    constexpr int t = decltype(tc)::v;
    constexpr int pr = xread(t), pa = xread(t - 3);
    if (t == PR1 + 3) __builtin_amdgcn_s_setprio(2);
    if (t == PR2 + 3) __builtin_amdgcn_s_setprio(1);
    if (t == PR3 + 3) __builtin_amdgcn_s_setprio(0);
    if (WF) waitlgkm<(t < 23 ? 2 : 24 - t) + xcount(t - 1) + xcount(t - 2)>(a[t == 24 ? 4 : t & 3]);
    if (t == 1) { rd64x2<0>(bv, ba); rd64x2<512>(uv, ba); }
    if constexpr (pr >= 0) {
      if constexpr (pr >= J0) waitvm<4 - pr + (TS >= qstep(pr)) + pcount(0, t)>();
      rd128<(pr & 3) * 8192>(xs[pr], pr == 4 ? sa : sb);
    }
    if constexpr (t + 3 < 25) rdA(IC<t + 3>{});
    if (!WF) waitlgkm<(t + 3 < 25 ? 3 : 24 - t) + xcount(t) + xcount(t - 1) + xcount(t - 2)>(a[t == 24 ? 4 : t & 3]);
    mma(tc);
    if (t == TSO) storeP(6, pac0, pac1, true);
    if (t == TP5) ldP(p5, rsP, l16, psrc(5));
    if (t == TP6A) ldP(p6a, rsP, l16, psrc(6));
    if (t == TP6B) ldP(p6b, rsP, l16, cp1);
    if (t == TP6C) ldP(p6c, rsP, l16, cp2);
    if (t == 3) {
      const int m = lane & 15;
      const unsigned one = (lane >> 4) == (m >> 2) ? 0x3f80u << (16 * (m & 1)) : 0u;
      const unsigned x0 = (m & 2) ? 0u : one, x1 = (m & 2) ? one : 0u;
      sel0 = __builtin_bit_cast(s16x8, u32x4{x0, x1, 0u, 0u});
      sel1 = __builtin_bit_cast(s16x8, u32x4{0u, 0u, x0, x1});
    }
    if constexpr (pa >= 0) {
      const f32x4 z = {0.f, 0.f, 0.f, 0.f};
      tie(xs[pa]);
      pv = u32x4{xs[pa][0] ^ t0, xs[pa][1] ^ t1, xs[pa][2] ^ t0, xs[pa][3] ^ t1};
      bad |= (pv[0] | pv[1]) | (pv[2] | pv[3]);
      sum0 = __builtin_amdgcn_mfma_f32_16x16x32_bf16(sel0, __builtin_bit_cast(s16x8, pv), pa == 0 ? z : sum0, 0, 0, 0);
      sum1 = __builtin_amdgcn_mfma_f32_16x16x32_bf16(sel1, __builtin_bit_cast(s16x8, pv), pa == 0 ? z : sum1, 0, 0, 0);
    }
    if (t == 5) {
      tie(uv);
#pragma unroll
      for (int e = 0; e < 4; ++e) { u[2 * e] = lo16(uv[e]); u[2 * e + 1] = hi16(uv[e]); tie(u[2 * e]); tie(u[2 * e + 1]); }
    }
    if (t == 6) {
      tie(bv);
#pragma unroll
      for (int e = 0; e < 4; ++e) { bb[2 * e] = lo16(bv[e]); bb[2 * e + 1] = hi16(bv[e]); tie(bb[2 * e]); tie(bb[2 * e + 1]); }
    }
    if (t == 8) {
      d = bb[0] * u[0];
#pragma unroll
      for (int e = 1; e < 8; ++e) d += bb[e] * u[e];
      tie(d);
    }
    fence();
  });
  if (__builtin_expect(__builtin_amdgcn_ballot_w64((bad & kTagBits) != 0u) == 0ull, 1)) { acc0 += sum0; acc1 += sum1; } else redo = 31u;
  if (__builtin_expect(untag<3>(pv, p5, t0, t1), 1)) addP(pv); else redo |= 32u;
  if (__builtin_expect(!untag<2>(pv, p6a, t0, t1), 0)) {
    _Pragma("unroll 1") for (int spin = 0; spin < (1 << 13); ++spin) {
      ldPr(p6a, rsP, l16, psrc(6));
      if (untag<2>(pv, p6b, t0, t1)) break;
      ldPr(p6b, rsP, l16, cp1);
      if (untag<2>(pv, p6c, t0, t1)) break;
      ldPr(p6c, rsP, l16, cp2);
      if (untag<2>(pv, p6a, t0, t1)) break;
    }
  }
  addP(pv);
  if (__builtin_expect(redo != 0, 0)) {
    _Pragma("unroll 1") for (int spin = 0; spin < (1 << 14) && redo; ++spin) {
      u32x4 g[6];
#define WD_POLL(j) "s_bitcmp1_b32 %8, " #j "\n\ts_cselect_b64 exec, -1, 0\n\tbuffer_load_dwordx4 %" #j ", %6, %7, %1" #j " offen " LSFX "\n\t"
      asm volatile(WD_POLL(5) WD_POLL(4) WD_POLL(3) WD_POLL(2) WD_POLL(1) WD_POLL(0)
                   "s_mov_b64 exec, -1\n\ts_waitcnt vmcnt(0)\n.Lwd_end:"
                   : "=&v"(g[0]), "=&v"(g[1]), "=&v"(g[2]), "=&v"(g[3]), "=&v"(g[4]), "=&v"(g[5])
                   : "v"(l16), "s"(rsP), "s"(redo), "s"(0), "s"(psrc(0)), "s"(psrc(1)), "s"(psrc(2)), "s"(psrc(3)), "s"(psrc(4)), "s"(psrc(5))
                   : "scc", "memory");
      auto take = [&](int j) { if ((redo >> j & 1) && untag<63>(pv, g[j], t0, t1)) { addP(pv); redo ^= 1u << j; } };
      take(5);
      if (redo & 31u) { take(4); take(3); take(2); take(1); take(0); }
    }
  }
  {
    auto mx = [](float x, float b) { return __builtin_amdgcn_fmed3f(x, -b, 3.0e38f); };
    float d1 = mx(acc1[0], bb[4]) * u[4];
#pragma unroll
    for (int e = 0; e < 4; ++e) d += mx(acc0[e], bb[e]) * u[e];
#pragma unroll
    for (int e = 1; e < 4; ++e) d1 += mx(acc1[e], bb[4 + e]) * u[4 + e];
    d += d1;
  }
  {
    auto r = __builtin_amdgcn_permlane32_swap(__float_as_uint(d), __float_as_uint(d), false, false);
    d = __uint_as_float(r[0]) + __uint_as_float(r[1]);
    auto r2 = __builtin_amdgcn_permlane16_swap(__float_as_uint(d), __float_as_uint(d), false, false);
    d = __uint_as_float(r2[0]) + __uint_as_float(r2[1]);
  }
  if (lane < 16) *(float*)(lds + kLdsRed + lane * 32 + w * 4) = d;
  waitlgkm<0>();
  bar();
  if (tid < 16) {
    const f32x4 ra = *(const f32x4*)(lds + kLdsRed + tid * 32), rb = *(const f32x4*)(lds + kLdsRed + tid * 32 + 16);
    const float z = *(const float*)(lds + kLdsWide + tid * 4) + ((ra[0] + ra[1]) + (ra[2] + ra[3])) + ((rb[0] + rb[1]) + (rb[2] + rb[3]));
    const float r = __builtin_amdgcn_rcpf(1.f + __expf(-z));
    out[mt * 128 + 16 * s + tid] = (unsigned short)(pack2(r, r) & 0xffffu);
  }
  if (w == 1) stFlag(tag, rsP, kWsEpoch + mt * 128 + s * 16);
  asm volatile("s_waitcnt vmcnt(0)" :: "v"(p6a), "v"(p6b), "v"(p6c) : "memory");
}

}

extern "C" void kernel_launch(void* const* d_in, const int* in_sizes, int n_in, void* d_out, int out_size,
                              void* d_ws, size_t ws_size, hipStream_t stream) {
  if (ws_size < kWsBytes) return;
  (void)hipFuncSetAttribute((const void*)wide_deep_kernel, hipFuncAttributeMaxDynamicSharedMemorySize, kLdsBytes);
  wide_deep_kernel<<<256, 512, kLdsBytes, stream>>>(
      (const int*)d_in[0], (const char*)d_in[1], (const char*)d_in[2], (const char*)d_in[3], (const char*)d_in[4],
      (const char*)d_in[5], (const char*)d_in[6], (const char*)d_in[7], (const char*)d_in[8], (unsigned short*)d_out,
      (char*)d_ws, (unsigned)in_sizes[1] * 2u, 0u);
}
```
